```python
import torch

def _softmax(x, axis=-1):
    return torch.softmax(x, dim=axis)

def _gelu(x):
    return torch.nn.functional.gelu(x, approximate='tanh')

def _silu(x):
    return torch.nn.functional.silu(x)

def _sigmoid(x):
    return torch.sigmoid(x)

def _layernorm(x, axis=-1, eps=1e-05):
    m = x.mean(dim=axis, keepdim=True)
    v = x.var(dim=axis, keepdim=True, unbiased=False)
    return (x - m) / torch.sqrt(v + eps)

def _scatter_add(tgt, idx, src, axis=0):
    return tgt.clone().index_add_(axis, idx, src)

def _segment_sum(src, seg, n):
    out = torch.zeros((n,) + src.shape[1:], dtype=src.dtype)
    return out.index_add_(0, seg, src)

def _pad(x, pw):
    flat = tuple((v for pair in reversed(pw) for v in pair))
    return torch.nn.functional.pad(x, flat)
kernel_name = 'standalone__tree_speculative_verify_tree_attention_over_candidate_token_tree__decode'

def setup_inputs():

    _w = lambda a: (lambda t: t.to(torch.bfloat16) if t.is_floating_point() else t)(torch.as_tensor(a))
    B, Hq, Hkv, T, C, D = (2, 32, 16, 1, 16384, 128)
    tree_mask = torch.zeros((B, T, T), dtype=float)
    for b in range(B):
        parent = torch.zeros(T, dtype=torch.int64)
        for i in range(1, T):
            parent[i] = int(torch.randint(0, i, (1,)).item())
        for i in range(T):
            j = i
            tree_mask[b, i, j] = 1.0
            while j > 0:
                j = parent[j]
                tree_mask[b, i, j] = 1.0
    return {'q': _w((lambda *s: torch.randn(*s, dtype=torch.bfloat16) * 0.8)(B, Hq, T, D)), 'k_cache': _w((lambda *s: torch.randn(*s, dtype=torch.bfloat16) * 0.8)(B, Hkv, C, D)), 'v_cache': _w((lambda *s: torch.randn(*s, dtype=torch.bfloat16) * 0.05)(B, Hkv, C, D)), 'k_tree': _w((lambda *s: torch.randn(*s, dtype=torch.bfloat16) * 0.8)(B, Hkv, T, D)), 'v_tree': _w((lambda *s: torch.randn(*s, dtype=torch.bfloat16) * 0.05)(B, Hkv, T, D)), 'tree_mask': _w(tree_mask)}

def reference(q, k_cache, v_cache, k_tree, v_tree, tree_mask):
    B, Hq, T, D = q.shape
    Hkv, C = (k_cache.shape[1], k_cache.shape[2])
    rep = Hq // Hkv
    kc = (lambda x, n, axis: torch.repeat_interleave(x, n, dim=axis))(k_cache, rep, axis=1)
    vc = (lambda x, n, axis: torch.repeat_interleave(x, n, dim=axis))(v_cache, rep, axis=1)
    kt = (lambda x, n, axis: torch.repeat_interleave(x, n, dim=axis))(k_tree, rep, axis=1)
    vt = (lambda x, n, axis: torch.repeat_interleave(x, n, dim=axis))(v_tree, rep, axis=1)
    k = torch.concatenate([kc, kt], axis=2)
    v = torch.concatenate([vc, vt], axis=2)
    scale = D ** (-0.5)
    scores = torch.einsum('bhqd,bhkd->bhqk', q, k) * scale
    ctx_mask = torch.ones((B, 1, T, C), dtype=scores.dtype)
    tm = tree_mask[:, None, :, :].clone()
    full_mask = torch.concatenate([ctx_mask, tm], axis=-1)
    scores = scores + (1.0 - full_mask) * -1000000000.0
    p = _softmax(scores, axis=-1)
    return torch.einsum('bhqk,bhkd->bhqd', p, v)
```

```cpp
#define WAVES 4
#define MAP 3
#define ROT 0
#include <hip/hip_runtime.h>
#include <hip/hip_bf16.h>
#include <cstdint>

constexpr int BATCH = 2, HQ = 32, HKV = 16, CTX = 16384, D = 128, G = HQ / HKV;
#define NSPLIT 8
#define NBUF 2
#define KV_NT 1
#define ILV 0
constexpr int PAGE = 16;
constexpr int NPAGES = CTX / PAGE;
constexpr unsigned TSTEP = 4096;
__device__ __forceinline__ unsigned pageoff(int w, int i, int rot) {
  constexpr int CPM = CTX / PAGE / NSPLIT - 1;
  return (unsigned)((4 * i + rot) & CPM) * 4096u + (unsigned)w * 1024u;
}
constexpr float SCALE = 0.088388347648318440f;
constexpr float LOG2E = 1.4426950408889634f;
constexpr int NROWS = BATCH * HQ;
constexpr size_t WS_PML = 0;
constexpr size_t WS_PO = 65536;

using bf16 = __hip_bfloat16;
using bf16x2 = __attribute__((ext_vector_type(2))) __bf16;
using u32x4 = __attribute__((ext_vector_type(4))) unsigned;
using f32x4 = __attribute__((ext_vector_type(4))) float;
using f32x2 = __attribute__((ext_vector_type(2))) float;

__device__ __forceinline__ float dot2(unsigned k, unsigned q, float acc) {
  return __builtin_amdgcn_fdot2_f32_bf16(__builtin_bit_cast(bf16x2, k), __builtin_bit_cast(bf16x2, q), acc, false);
}
template <int CTRL> __device__ __forceinline__ float dpp(float x) {
  return __builtin_bit_cast(float, __builtin_amdgcn_mov_dpp(__builtin_bit_cast(int, x), CTRL, 0xf, 0xf, true));
}
constexpr int XOR1 = 0xB1, XOR2 = 0x4E, XOR7 = 0x141, XOR8 = 0x128, ROR4 = 0x124, NEWBCAST = 0x150;
__device__ __forceinline__ float xrow16_max(float x) {
  auto s = __builtin_amdgcn_permlane16_swap(__float_as_uint(x), __float_as_uint(x), false, false);
  x = fmaxf(__uint_as_float(s[0]), __uint_as_float(s[1]));
  auto t = __builtin_amdgcn_permlane32_swap(__float_as_uint(x), __float_as_uint(x), false, false);
  return fmaxf(__uint_as_float(t[0]), __uint_as_float(t[1]));
}
__device__ __forceinline__ float xrow16_sum(float x) {
  auto s = __builtin_amdgcn_permlane16_swap(__float_as_uint(x), __float_as_uint(x), false, false);
  x = __uint_as_float(s[0]) + __uint_as_float(s[1]);
  auto t = __builtin_amdgcn_permlane32_swap(__float_as_uint(x), __float_as_uint(x), false, false);
  return __uint_as_float(t[0]) + __uint_as_float(t[1]);
}
__device__ __forceinline__ u32x4 ldkv(const unsigned char* p) {
  return __builtin_nontemporal_load(reinterpret_cast<const u32x4*>(p));
}

__global__ void __launch_bounds__(WAVES * 64) attn_split(const bf16* __restrict__ q, const bf16* __restrict__ k,
                                                         const bf16* __restrict__ v, unsigned char* __restrict__ ws) {
  __shared__ __attribute__((aligned(16))) float lds[WAVES * G * (4 + D)];
  const int s = blockIdx.x % NSPLIT, bh = blockIdx.x / NSPLIT;
  const int tid = threadIdx.x, lane = tid & 63, wave = __builtin_amdgcn_readfirstlane(tid >> 6), r = lane >> 4, c = lane & 15;
  constexpr float C2 = SCALE * LOG2E;
  u32x4 qpk[G];
#pragma unroll
  for (int g = 0; g < G; ++g) qpk[g] = *reinterpret_cast<const u32x4*>(q + ((size_t)bh * G + g) * D + 8 * c);
  constexpr int CP = NPAGES / NSPLIT;
  const int rot = (int)((blockIdx.x * (unsigned)ROT) % (unsigned)CP);
  const size_t base = ((size_t)bh * CTX + (size_t)s * CP * PAGE) * D * 2 + (size_t)lane * 16;
  const unsigned char* kp = reinterpret_cast<const unsigned char*>(k) + base;
  const unsigned char* vp = reinterpret_cast<const unsigned char*>(v) + base;
  u32x4 Kb[4], Vb[4];
  {
    const unsigned o = pageoff(wave, 0, rot);
#pragma unroll
    for (int t = 0; t < 4; ++t) Kb[t] = ldkv(kp + o + t * TSTEP);
#pragma unroll
    for (int t = 0; t < 4; ++t) Vb[t] = ldkv(vp + o + t * TSTEP);
  }
  float m = -INFINITY, l = 0.f, acc[G][8];
#pragma unroll
  for (int g = 0; g < G; ++g)
#pragma unroll
    for (int j = 0; j < 8; ++j) acc[g][j] = 0.f;

  auto step = [&](u32x4 (&Kc)[4], u32x4 (&Vc)[4], const unsigned char* kn, const unsigned char* vn, bool issue) {
    float x[8];
#pragma unroll
    for (int t = 0; t < 4; ++t)
#pragma unroll
      for (int g = 0; g < G; ++g) {
        float d = 0.f;
#pragma unroll
        for (int j = 0; j < 4; ++j) d = dot2(Kc[t][j], qpk[g][j], d);
        x[t * 2 + g] = d;
      }
    if (issue) {
      asm volatile("" : "+v"(x[0]), "+v"(x[1]), "+v"(x[2]), "+v"(x[3]), "+v"(x[4]), "+v"(x[5]), "+v"(x[6]), "+v"(x[7]) :: "memory");
      __builtin_amdgcn_sched_barrier(0);
#pragma unroll
      for (int t = 0; t < 4; ++t) Kc[t] = ldkv(kn + t * TSTEP);
      __builtin_amdgcn_sched_barrier(0);
    }
#pragma unroll
    for (int h = 0; h < 4; ++h) { const float t1 = x[h] + dpp<XOR8>(x[h]), t2 = x[h + 4] + dpp<XOR8>(x[h + 4]); x[h] = (c & 8) ? t2 : t1; }
#pragma unroll
    for (int h = 0; h < 2; ++h) { const float t1 = x[h] + dpp<XOR7>(x[h]), t2 = x[h + 2] + dpp<XOR7>(x[h + 2]); x[h] = (c & 4) ? t2 : t1; }
    { const float t1 = x[0] + dpp<XOR2>(x[0]), t2 = x[1] + dpp<XOR2>(x[1]); x[0] = (c & 2) ? t2 : t1; }
    const float sc = (x[0] + dpp<XOR1>(x[0])) * C2;
    float pm = fmaxf(sc, dpp<ROR4>(sc)); pm = fmaxf(pm, dpp<XOR8>(pm));
    const float mn = fmaxf(m, pm);
    const float alpha = __builtin_amdgcn_exp2f(m - mn), p = __builtin_amdgcn_exp2f(sc - mn);
    l = fmaf(l, alpha, p); m = mn;
    const float a0 = dpp<0x00>(alpha), a1 = dpp<0xAA>(alpha);
#pragma unroll
    for (int j = 0; j < 8; ++j) { acc[0][j] *= a0; acc[1][j] *= a1; }
#pragma unroll
    for (int t = 0; t < 4; ++t) {
      float vf[8];
#pragma unroll
      for (int j = 0; j < 4; ++j) { vf[2 * j] = __builtin_bit_cast(float, Vc[t][j] << 16); vf[2 * j + 1] = __builtin_bit_cast(float, Vc[t][j] & 0xffff0000u); }
      const float p0 = t == 0 ? dpp<NEWBCAST + 0>(p) : t == 1 ? dpp<NEWBCAST + 4>(p) : t == 2 ? dpp<NEWBCAST + 8>(p) : dpp<NEWBCAST + 12>(p);
      const float p1 = t == 0 ? dpp<NEWBCAST + 2>(p) : t == 1 ? dpp<NEWBCAST + 6>(p) : t == 2 ? dpp<NEWBCAST + 10>(p) : dpp<NEWBCAST + 14>(p);
#pragma unroll
      for (int j = 0; j < 8; ++j) { acc[0][j] = fmaf(p0, vf[j], acc[0][j]); acc[1][j] = fmaf(p1, vf[j], acc[1][j]); }
    }
    if (issue) {
      asm volatile("" : "+v"(acc[0][0]), "+v"(acc[0][1]), "+v"(acc[0][2]), "+v"(acc[0][3]), "+v"(acc[0][4]), "+v"(acc[0][5]), "+v"(acc[0][6]), "+v"(acc[0][7]),
                   "+v"(acc[1][0]), "+v"(acc[1][1]), "+v"(acc[1][2]), "+v"(acc[1][3]), "+v"(acc[1][4]), "+v"(acc[1][5]), "+v"(acc[1][6]), "+v"(acc[1][7]) :: "memory");
      __builtin_amdgcn_sched_barrier(0);
#pragma unroll
      for (int t = 0; t < 4; ++t) Vc[t] = ldkv(vn + t * TSTEP);
      __builtin_amdgcn_sched_barrier(0);
    }
  };

  static_assert(WAVES == 4, "");
  for (int i = 1; i < CP / WAVES; ++i) {
    const unsigned o = pageoff(wave, i, rot);
    step(Kb, Vb, kp + o, vp + o, true);
  }
  step(Kb, Vb, kp, vp, false);

  const float M = xrow16_max(m);
  const float scl = __builtin_amdgcn_exp2f(m - M);
  l *= scl;
  { float t = l; t += dpp<ROR4>(t); t += dpp<XOR8>(t); l = xrow16_sum(t); }
  const float s0 = dpp<0x00>(scl), s1 = dpp<0xAA>(scl);
#pragma unroll
  for (int j = 0; j < 8; ++j) { acc[0][j] = xrow16_sum(acc[0][j] * s0); acc[1][j] = xrow16_sum(acc[1][j] * s1); }
  if (r == 0) {
#pragma unroll
    for (int g = 0; g < G; ++g) {
      float* rec = lds + (wave * G + g) * (4 + D);
      *reinterpret_cast<f32x4*>(rec + 4 + 8 * c) = f32x4{acc[g][0], acc[g][1], acc[g][2], acc[g][3]};
      *reinterpret_cast<f32x4*>(rec + 4 + 8 * c + 4) = f32x4{acc[g][4], acc[g][5], acc[g][6], acc[g][7]};
      if (c == 2 * g) { rec[0] = M; rec[1] = l; }
    }
  }
  __syncthreads();
  if (wave < G) {
    const int g = wave, d0 = 2 * lane;
    float MM = -INFINITY;
#pragma unroll
    for (int w = 0; w < WAVES; ++w) MM = fmaxf(MM, lds[(w * G + g) * (4 + D)]);
    float L = 0.f, o0 = 0.f, o1 = 0.f;
#pragma unroll
    for (int w = 0; w < WAVES; ++w) {
      const float* rec = lds + (w * G + g) * (4 + D);
      const float wt = __builtin_amdgcn_exp2f(rec[0] - MM);
      L = fmaf(wt, rec[1], L); o0 = fmaf(wt, rec[4 + d0], o0); o1 = fmaf(wt, rec[4 + d0 + 1], o1);
    }
    const size_t slot = ((size_t)bh * G + g) * NSPLIT + s;
    float* pml = reinterpret_cast<float*>(ws + WS_PML);
    float* po = reinterpret_cast<float*>(ws + WS_PO);
    *reinterpret_cast<f32x2*>(po + slot * D + d0) = f32x2{o0, o1};
    if (lane == 0) *reinterpret_cast<f32x2*>(pml + 2 * slot) = f32x2{MM, L};
  }
}

__global__ void __launch_bounds__(64) attn_combine(const unsigned char* __restrict__ ws, const bf16* __restrict__ q, const bf16* __restrict__ kt,
                                                   const bf16* __restrict__ vt, const bf16* __restrict__ tmask, bf16* __restrict__ out) {
  const int row = blockIdx.x, lane = threadIdx.x, d0 = 2 * lane;
  const int b = row / HQ, bh = row / G;
  const float* pml = reinterpret_cast<const float*>(ws + WS_PML) + (size_t)row * NSPLIT * 2;
  const float* po = reinterpret_cast<const float*>(ws + WS_PO) + (size_t)row * NSPLIT * D;
  const unsigned qq = *reinterpret_cast<const unsigned*>(q + (size_t)row * D + d0);
  const unsigned kk = *reinterpret_cast<const unsigned*>(kt + (size_t)bh * D + d0);
  const unsigned vv = *reinterpret_cast<const unsigned*>(vt + (size_t)bh * D + d0);
  const float mk = __bfloat162float(tmask[b]);
  f32x2 ml[NSPLIT], a[NSPLIT];
#pragma unroll
  for (int s = 0; s < NSPLIT; ++s) { ml[s] = *reinterpret_cast<const f32x2*>(pml + 2 * s); a[s] = *reinterpret_cast<const f32x2*>(po + (size_t)s * D + d0); }
  float d = dot2(kk, qq, 0.f);
#pragma unroll
  for (int o = 32; o >= 1; o >>= 1) d += __shfl_xor(d, o, 64);
  const float st = (d * SCALE + (1.0f - mk) * -1000000000.0f) * LOG2E;
  float M = st;
#pragma unroll
  for (int s = 0; s < NSPLIT; ++s) M = fmaxf(M, ml[s][0]);
  const float pt = __builtin_amdgcn_exp2f(st - M);
  float L = pt, o0 = pt * __builtin_bit_cast(float, vv << 16), o1 = pt * __builtin_bit_cast(float, vv & 0xffff0000u);
#pragma unroll
  for (int s = 0; s < NSPLIT; ++s) {
    const float w = __builtin_amdgcn_exp2f(ml[s][0] - M);
    L = fmaf(w, ml[s][1], L); o0 = fmaf(w, a[s][0], o0); o1 = fmaf(w, a[s][1], o1);
  }
  const float inv = 1.0f / L;
  __hip_bfloat162 o2; o2.x = __float2bfloat16(o0 * inv); o2.y = __float2bfloat16(o1 * inv);
  *reinterpret_cast<__hip_bfloat162*>(out + (size_t)row * D + d0) = o2;
}

extern "C" void kernel_launch(void* const* d_in, const int* in_sizes, int n_in, void* d_out, int out_size, void* d_ws, size_t ws_size,
                              hipStream_t stream) {
  const bf16* q = (const bf16*)d_in[0];
  const bf16* kc = (const bf16*)d_in[1];
  const bf16* vc = (const bf16*)d_in[2];
  const bf16* kt = (const bf16*)d_in[3];
  const bf16* vt = (const bf16*)d_in[4];
  const bf16* tm = (const bf16*)d_in[5];
  attn_split<<<dim3(BATCH * HKV * NSPLIT), dim3(WAVES * 64), 0, stream>>>(q, kc, vc, (unsigned char*)d_ws);
  attn_combine<<<dim3(NROWS), dim3(64), 0, stream>>>((const unsigned char*)d_ws, q, kt, vt, tm, (bf16*)d_out);
}
```
